```python
import torch

def _softmax(x, axis=-1):
    return torch.softmax(x, dim=axis)
def _gelu(x):
    return torch.nn.functional.gelu(x, approximate="tanh")
def _silu(x):
    return torch.nn.functional.silu(x)
def _sigmoid(x):
    return torch.sigmoid(x)
def _layernorm(x, axis=-1, eps=1e-5):
    m = x.mean(dim=axis, keepdim=True)
    v = x.var(dim=axis, keepdim=True, unbiased=False)
    return (x - m) / torch.sqrt(v + eps)
def _scatter_add(tgt, idx, src, axis=0):
    return tgt.clone().index_add_(axis, idx, src)
def _segment_sum(src, seg, n):
    out = torch.zeros((n,) + src.shape[1:], dtype=src.dtype)
    return out.index_add_(0, seg, src)
def _pad(x, pw):
    flat = tuple(v for pair in reversed(pw) for v in pair)
    return torch.nn.functional.pad(x, flat)

kernel_name = 'sequential_preLN__gqa_block_cached__mixer_channel_mlp__decode'

def setup_inputs():

    _w = (lambda a: (lambda t: t.to(torch.bfloat16) if t.is_floating_point() else t)(torch.as_tensor(a)))
    def _A_setup():
        B, Sq, D = 4, 1, 512
        Hq, Hkv, Sk, Dh = 32, 8, 4096, 128
        assert Hq % Hkv == 0, (Hq, Hkv)
        return {
            "x": _w((lambda *s: torch.randn(*s, dtype=torch.bfloat16) * 0.05)(B, Sq, D)),
            "k_cache": _w((lambda *s: torch.randn(*s, dtype=torch.bfloat16) * 3.2)(B, Hkv, Sk, Dh)),
            "v_cache": _w((lambda *s: torch.randn(*s, dtype=torch.bfloat16) * 0.4)(B, Hkv, Sk, Dh)),
            "wq": _w((lambda *s: torch.randn(*s, dtype=torch.bfloat16) * 3.2)(D, Hq * Dh) * (D ** -0.5)),
            "wo": _w((lambda *s: torch.randn(*s, dtype=torch.bfloat16) * 0.4)(Hq * Dh, D) * ((Hq * Dh) ** -0.5)),
        }
    _A_in = _A_setup()

    def _B_setup():
        B, S, D, F = 4, 1, 512, 1792
        return {
            "x": _w((lambda *s: torch.randn(*s, dtype=torch.bfloat16) * 0.05)(B, S, D)),
            "w1": _w((lambda *s: torch.randn(*s, dtype=torch.bfloat16) * 3.2)(D, F) * (D ** -0.5)),
            "b1": _w((lambda *s: torch.randn(*s, dtype=torch.bfloat16) * 3.2)(F)),
            "w2": _w((lambda *s: torch.randn(*s, dtype=torch.bfloat16) * 0.4)(F, D) * (F ** -0.5)),
            "b2": _w((lambda *s: torch.randn(*s, dtype=torch.bfloat16) * 0.8)(D)),
        }
    _B_in = _B_setup()

    return {
        "A_x": _A_in["x"],
        "A_k_cache": _A_in["k_cache"],
        "A_v_cache": _A_in["v_cache"],
        "A_wq": _A_in["wq"],
        "A_wo": _A_in["wo"],
        "B_x": _B_in["x"],
        "B_w1": _B_in["w1"],
        "B_b1": _B_in["b1"],
        "B_w2": _B_in["w2"],
        "B_b2": _B_in["b2"],
    }

def reference(A_x, A_k_cache, A_v_cache, A_wq, A_wo, B_x, B_w1, B_b1, B_w2, B_b2):
    def _A_ref(x, k_cache, v_cache, wq, wo):
        B, Sq, D = x.shape
        Hq, Dh = 32, 128
        Hkv = k_cache.shape[1]
        rep = Hq // Hkv
        q = (lambda x, *d: x.permute(*d))((x @ wq).reshape(B, Sq, Hq, Dh), 0, 2, 1, 3)
        k = (lambda x, n, axis: torch.repeat_interleave(x, n, dim=axis))(k_cache, rep, axis=1)
        v = (lambda x, n, axis: torch.repeat_interleave(x, n, dim=axis))(v_cache, rep, axis=1)
        scores = torch.einsum("bhqd,bhkd->bhqk", q, k) * (Dh ** -0.5)
        p = _softmax(scores, axis=-1)
        o = torch.einsum("bhqk,bhkd->bhqd", p, v)
        return (lambda x, *d: x.permute(*d))(o, 0, 2, 1, 3).reshape(B, Sq, Hq * Dh) @ wo

    def _B_ref(x, w1, b1, w2, b2):
        y = _layernorm(x, axis=-1)
        h = _gelu(torch.einsum("bsd,df->bsf", y, w1) + b1)
        return torch.einsum("bsf,fd->bsd", h, w2) + b2

    x = A_x
    h = x + _A_ref(_layernorm(x, axis=-1), A_k_cache, A_v_cache, A_wq, A_wo)
    h2 = h + _B_ref(_layernorm(h, axis=-1), B_w1, B_b1, B_w2, B_b2)
    return h2
```

```cpp
#include <hip/hip_runtime.h>
#include <hip/hip_bf16.h>
typedef unsigned short u16;
__device__ __forceinline__ float b2f(u16 v){ return __uint_as_float(((unsigned)v)<<16); }
__device__ __forceinline__ float rbf(float f){ unsigned u=__float_as_uint(f); u = (u + 0x7FFF + ((u>>16)&1)) & 0xFFFF0000u; return __uint_as_float(u);}
__device__ __forceinline__ u16 f2b(float f){ return (u16)(__float_as_uint(rbf(f))>>16);}
__device__ float block_sum(float v, float* sm){
  for(int o=32;o>0;o>>=1) v += __shfl_xor(v,o);
  int w=threadIdx.x>>6; __syncthreads(); if((threadIdx.x&63)==0) sm[w]=v; __syncthreads();
  float s=0; for(int i=0;i<(int)(blockDim.x>>6);i++) s+=sm[i]; return s; }
__device__ void ln512(float& a, float& b, float* sm){
  float m = block_sum(a+b, sm)*(1.f/512.f);
  float da=a-m, db=b-m;
  float v = block_sum(da*da+db*db, sm)*(1.f/512.f);
  float mb=rbf(m), vb=rbf(v);
  float sd = rbf(sqrtf(rbf(vb+1e-5f)));
  a = rbf(rbf(a-mb)/sd); b = rbf(rbf(b-mb)/sd);
}
__global__ void k_q(const u16* x, const u16* wq, float* q){
  __shared__ float sm[8]; __shared__ float xn[512];
  int b=blockIdx.y, t=threadIdx.x;
  float a=b2f(x[b*512+t]), c=b2f(x[b*512+256+t]); ln512(a,c,sm); xn[t]=a; xn[256+t]=c; __syncthreads();
  int col=blockIdx.x*256+t; float acc=0;
  for(int k=0;k<512;k++) acc += xn[k]*b2f(wq[(size_t)k*4096+col]);
  q[b*4096+col]=rbf(acc);
}
__global__ void k_attn(const float* q, const u16* K, const u16* V, float* o){
  __shared__ float s[4096]; __shared__ float sm[8]; __shared__ float qs[128]; __shared__ float red[2][128];
  int h=blockIdx.x,b=blockIdx.y,t=threadIdx.x,g=h>>2;
  if(t<128) qs[t]=q[b*4096+h*128+t]; __syncthreads();
  const u16* Kb=K+(size_t)(b*8+g)*4096*128; const u16* Vb=V+(size_t)(b*8+g)*4096*128;
  float mx=-INFINITY;
  for(int k=t;k<4096;k+=256){ float a=0; for(int d=0;d<128;d++) a+=qs[d]*b2f(Kb[(size_t)k*128+d]); a=rbf(rbf(a)*0.08838834764831845f); s[k]=a; mx=fmaxf(mx,a);}
  for(int of=32;of>0;of>>=1) mx=fmaxf(mx,__shfl_xor(mx,of));
  __syncthreads(); if((t&63)==0) sm[t>>6]=mx; __syncthreads(); mx=fmaxf(fmaxf(sm[0],sm[1]),fmaxf(sm[2],sm[3]));
  float l=0; for(int k=t;k<4096;k+=256){ float e=__expf(s[k]-mx); s[k]=e; l+=e; }
  l=block_sum(l,sm); __syncthreads();
  int d=t&127, half=t>>7; float acc=0;
  for(int k=half*2048;k<half*2048+2048;k++) acc += rbf(s[k]/l)*b2f(Vb[(size_t)k*128+d]);
  red[half][d]=acc; __syncthreads();
  if(t<128) o[b*4096+h*128+t]=rbf(red[0][t]+red[1][t]);
}
__global__ void k_wo(const float* o, const u16* wo, const u16* x, float* h){
  __shared__ float red[4][64];
  int b=blockIdx.y,t=threadIdx.x,c=blockIdx.x*64+(t&63),part=t>>6; float acc=0;
  for(int k=part*1024;k<part*1024+1024;k++) acc+=o[b*4096+k]*b2f(wo[(size_t)k*512+c]);
  red[part][t&63]=acc; __syncthreads();
  if(t<64){ float a=rbf(red[0][t]+red[1][t]+red[2][t]+red[3][t]); h[b*512+c]=rbf(b2f(x[b*512+c])+a);}
}
__global__ void k_w1(const float* h, const u16* w1, const u16* b1, float* hid){
  __shared__ float sm[8]; __shared__ float y[512];
  int b=blockIdx.y,t=threadIdx.x; float a=h[b*512+t], c=h[b*512+256+t];
  ln512(a,c,sm); ln512(a,c,sm); y[t]=a; y[256+t]=c; __syncthreads();
  int f=blockIdx.x*256+t; float acc=0;
  for(int k=0;k<512;k++) acc+=y[k]*b2f(w1[(size_t)k*1792+f]);
  float u=rbf(rbf(acc)+b2f(b1[f]));
  float g=0.5f*u*(1.f+tanhf(0.7978845608028654f*(u+0.044715f*u*u*u)));
  hid[b*1792+f]=rbf(g);
}
__global__ void k_w2(const float* hid, const u16* w2, const u16* b2, const float* h, u16* out){
  __shared__ float red[4][64];
  int b=blockIdx.y,t=threadIdx.x,c=blockIdx.x*64+(t&63),part=t>>6; float acc=0;
  for(int k=part*448;k<part*448+448;k++) acc+=hid[b*1792+k]*b2f(w2[(size_t)k*512+c]);
  red[part][t&63]=acc; __syncthreads();
  if(t<64){ float a=rbf(red[0][t]+red[1][t]+red[2][t]+red[3][t]); a=rbf(a+b2f(b2[c])); out[b*512+c]=f2b(h[b*512+c]+a);}
}
extern "C" void kernel_launch(void* const* d_in, const int* in_sizes, int n_in, void* d_out, int out_size, void* d_ws, size_t ws_size, hipStream_t stream){
  const u16* x=(const u16*)d_in[0]; const u16* K=(const u16*)d_in[1]; const u16* V=(const u16*)d_in[2];
  const u16* wq=(const u16*)d_in[3]; const u16* wo=(const u16*)d_in[4];
  const u16* w1=(const u16*)d_in[6]; const u16* b1=(const u16*)d_in[7]; const u16* w2=(const u16*)d_in[8]; const u16* b2=(const u16*)d_in[9];
  float* ws=(float*)d_ws; float* q=ws; float* o=ws+16384; float* h=ws+32768; float* hid=ws+36864;
  k_q<<<dim3(16,4),256,0,stream>>>(x,wq,q);
  k_attn<<<dim3(32,4),256,0,stream>>>(q,K,V,o);
  k_wo<<<dim3(8,4),256,0,stream>>>(o,wo,x,h);
  k_w1<<<dim3(7,4),256,0,stream>>>(h,w1,b1,hid);
  k_w2<<<dim3(8,4),256,0,stream>>>(hid,w2,b2,h,(u16*)d_out);
}
```

```cpp
#include <hip/hip_runtime.h>
#include <stdint.h>

typedef unsigned u32;
typedef unsigned long long u64;
typedef unsigned short u16;
typedef u32 u32x4 __attribute__((ext_vector_type(4)));
typedef u32 u32x2 __attribute__((ext_vector_type(2)));
typedef float f32x4 __attribute__((ext_vector_type(4)));
typedef __bf16 bf16x8 __attribute__((ext_vector_type(8)));
typedef __bf16 bf16x2 __attribute__((ext_vector_type(2)));
typedef float f32x2 __attribute__((ext_vector_type(2)));
typedef short s16x4 __attribute__((ext_vector_type(4)));
#define LAS __attribute__((address_space(3)))
#define GAS __attribute__((address_space(1)))
#define RLX_AGENT __ATOMIC_RELAXED, __HIP_MEMORY_SCOPE_AGENT

constexpr u32 WS_EPOCH = 0, WS_FLAG = 4096, WS_Q = 8192, WS_O = WS_Q + 131072, WS_ML = WS_O + 524288,
              WS_A = WS_ML + 8192, WS_Y = WS_A + 65536, WS_END = WS_Y + 4 * 56 * 2048;
constexpr u32 L_WO = 0, L_W1 = 65536, L_W2 = 98304, L_WQ = 131072, L_MRG = L_WQ, L_XN = 147456, L_QS = 151552,
              L_QP = 152576, L_MX = 153600, L_LX = 153728, L_WP = 153856, L_HB = 155904, L_HP = 157952,
              L_FLG = 158976, L_END = 159040;
constexpr u32 SENT = 0xFFFFFFFFu;
constexpr int SPIN_CAP = 1 << 16;
constexpr float LOG2E = 1.4426950408889634f;

#define STAMP(i) do {} while (0)
#define STAMP_DECL
#define STAMP_OUT() do {} while (0)

__device__ __forceinline__ float asf(u32 u) { return __builtin_bit_cast(float, u); }
__device__ __forceinline__ u32 asu(float f) { return __builtin_bit_cast(u32, f); }
__device__ __forceinline__ u32 pk2(float lo, float hi) {
  f32x2 v = {lo, hi};
  return __builtin_bit_cast(u32, __builtin_convertvector(v, bf16x2));
}
__device__ __forceinline__ float rbf(float f) { return asf(pk2(f, f) << 16); }
__device__ __forceinline__ float blo(u32 u) { return asf(u << 16); }
__device__ __forceinline__ float bhi(u32 u) { return asf(u & 0xffff0000u); }
template <int CTRL> __device__ __forceinline__ float dpp(float x) {
  return __builtin_bit_cast(float, __builtin_amdgcn_mov_dpp(__builtin_bit_cast(int, x), CTRL, 0xf, 0xf, true));
}
constexpr int XOR1 = 0xB1, XOR2 = 0x4E, XOR7 = 0x141, ROR8 = 0x128;
__device__ __forceinline__ float rows_sum(float x) {
  auto s = __builtin_amdgcn_permlane16_swap(asu(x), asu(x), false, false);
  x = asf(s[0]) + asf(s[1]);
  auto t = __builtin_amdgcn_permlane32_swap(asu(x), asu(x), false, false);
  return asf(t[0]) + asf(t[1]);
}
__device__ __forceinline__ float rows_max(float x) {
  auto s = __builtin_amdgcn_permlane16_swap(asu(x), asu(x), false, false);
  x = fmaxf(asf(s[0]), asf(s[1]));
  auto t = __builtin_amdgcn_permlane32_swap(asu(x), asu(x), false, false);
  return fmaxf(asf(t[0]), asf(t[1]));
}
__device__ __forceinline__ float wave_sum(float x) {
  x += dpp<XOR1>(x); x += dpp<XOR2>(x); x += dpp<XOR7>(x); x += dpp<ROR8>(x);
  return rows_sum(x);
}
__device__ __forceinline__ float rows_tr_sum(float v0, float v1, float v2, float v3) {
  auto a = __builtin_amdgcn_permlane32_swap(asu(v0), asu(v2), false, false);
  auto b = __builtin_amdgcn_permlane32_swap(asu(v1), asu(v3), false, false);
  const float d = asf(a[0]) + asf(a[1]), s = asf(b[0]) + asf(b[1]);
  auto c = __builtin_amdgcn_permlane16_swap(asu(d), asu(s), false, false);
  return asf(c[0]) + asf(c[1]);
}
__device__ __forceinline__ f32x4 mfma(u32x4 a, u32x4 b, f32x4 c) {
  return __builtin_amdgcn_mfma_f32_16x16x32_bf16(__builtin_bit_cast(bf16x8, a), __builtin_bit_cast(bf16x8, b), c, 0, 0, 0);
}
__device__ __forceinline__ u32x4 tr_frag(LAS unsigned char* sm, u32 a0, u32 a1) {
  const s16x4 lo = __builtin_amdgcn_ds_read_tr16_b64_v4i16((LAS s16x4*)(sm + a0));
  const s16x4 hi = __builtin_amdgcn_ds_read_tr16_b64_v4i16((LAS s16x4*)(sm + a1));
  const u32x2 l = __builtin_bit_cast(u32x2, lo), h = __builtin_bit_cast(u32x2, hi);
  return u32x4{l[0], l[1], h[0], h[1]};
}
#define WG_BARRIER() asm volatile("s_waitcnt lgkmcnt(0)\n\ts_barrier" ::: "memory")
#define CFENCE() asm volatile("" ::: "memory")
__device__ __forceinline__ void lds_wait(LAS unsigned char* sm, u32 off, u32 target) {
#pragma unroll 1
  for (int it = 0; it < (SPIN_CAP << 4); ++it) {
    if (*(volatile LAS u32*)(sm + off) >= target) break;
    __builtin_amdgcn_s_sleep(1);
  }
  CFENCE();
}
__device__ __forceinline__ void lds_signal(LAS unsigned char* sm, u32 off, int lane) {
  asm volatile("s_waitcnt lgkmcnt(0)" ::: "memory");
  if (lane == 0) __hip_atomic_fetch_add((LAS u32*)(sm + off), 1u, __ATOMIC_RELAXED, __HIP_MEMORY_SCOPE_WORKGROUP);
  CFENCE();
}
__device__ __forceinline__ void glds(const void* g, LAS unsigned char* l) {
  __builtin_amdgcn_global_load_lds((const GAS void*)g, (LAS void*)l, 16, 0, 2);
}
__device__ __forceinline__ u32 umax4(u32x4 v) { return max(max(v[0], v[1]), max(v[2], v[3])); }
__device__ __forceinline__ u32 clean(float f) { return min(asu(f), SENT - 1u); }
__device__ __forceinline__ void ln8(float (&v)[8]) {
  float s = ((v[0] + v[1]) + (v[2] + v[3])) + ((v[4] + v[5]) + (v[6] + v[7]));
  const float mean = wave_sum(s) * (1.f / 512.f);
  float q = 0.f;
#pragma unroll
  for (int j = 0; j < 8; ++j) { const float d = v[j] - mean; q = fmaf(d, d, q); }
  const float var = wave_sum(q) * (1.f / 512.f);
  const float mb = rbf(mean);
  const float sd = rbf(__builtin_sqrtf(rbf(rbf(var) + 1e-5f)));
  const float inv = __builtin_amdgcn_rcpf(sd);
#pragma unroll
  for (int j = 0; j < 8; j += 2) {
    const u32 d = pk2(v[j] - mb, v[j + 1] - mb);
    const u32 y = pk2(blo(d) * inv, bhi(d) * inv);
    v[j] = blo(y); v[j + 1] = bhi(y);
  }
}

__global__ void __launch_bounds__(768, 3)
fused_decode(const u16* __restrict__ x, const u16* __restrict__ Kc, const u16* __restrict__ Vc, const u16* __restrict__ wq,
             const u16* __restrict__ wo, const u16* __restrict__ w1, const u16* __restrict__ b1, const u16* __restrict__ w2,
             const u16* __restrict__ b2, u16* __restrict__ out, unsigned char* ws) {
  extern __shared__ __attribute__((aligned(16))) unsigned char smem_[];
  LAS unsigned char* sm = (LAS unsigned char*)smem_;
  const int bid = blockIdx.x, g = bid & 7, r = bid >> 3, b = r >> 3, s = r & 7;
  const bool mlp = s < 7;
  const int f = 7 * g + s, slot = (b * 8 + g) * 8 + s;
  const int lane = threadIdx.x & 63, wv = __builtin_amdgcn_readfirstlane(threadIdx.x >> 6);
  const int m = lane & 15, G = lane >> 4;
  const __amdgpu_buffer_rsrc_t rs = __builtin_amdgcn_make_buffer_rsrc(ws, 0, WS_END, 0x00020000);
  STAMP_DECL
  STAMP(0);

  if (wv < 4) {
    const int a = wv;
    __builtin_amdgcn_s_setprio(3);
    if (a == 0 && lane < 4) *(LAS u32*)(sm + L_FLG + 4 * lane) = 0u;
    const u64 ep = __hip_atomic_load((GAS u64*)(ws + WS_EPOCH), RLX_AGENT);
    u32x4 xr[4];
#pragma unroll
    for (int i = 0; i < 4; ++i) xr[i] = *(const u32x4*)(x + i * 512 + lane * 8);
#pragma unroll
    for (int i = 0; i < 4; ++i)
      glds(wq + (size_t)(128 * a + 32 * i + (lane >> 1)) * 4096 + 512 * g + 16 * r + 8 * (lane & 1), sm + L_WQ + (128 * a + 32 * i) * 32);
    __builtin_amdgcn_sched_barrier(0);
    WG_BARRIER();
    __builtin_amdgcn_sched_barrier(0);
#pragma unroll
    for (int i = 0; i < 4; ++i) {
      float v[8];
#pragma unroll
      for (int j = 0; j < 4; ++j) { v[2 * j] = blo(xr[i][j]); v[2 * j + 1] = bhi(xr[i][j]); }
      ln8(v);
      if (G == a) *(LAS u32x4*)(sm + L_XN + i * 1024 + lane * 16) = u32x4{pk2(v[0], v[1]), pk2(v[2], v[3]), pk2(v[4], v[5]), pk2(v[6], v[7])};
    }
    asm volatile("s_waitcnt vmcnt(0) lgkmcnt(0)" ::: "memory");
    STAMP(1);
    f32x4 qa = {0.f, 0.f, 0.f, 0.f};
#pragma unroll
    for (int ks = 0; ks < 4; ++ks) {
      const int k0 = 128 * a + 32 * ks + 4 * G;
      const u32x2 a0 = *(LAS u32x2*)(sm + L_XN + (m & 3) * 1024 + k0 * 2), a1 = *(LAS u32x2*)(sm + L_XN + (m & 3) * 1024 + (k0 + 16) * 2);
      const u32 ta = L_WQ + (k0 + (m >> 2)) * 32 + 8 * (m & 3);
      qa = mfma(u32x4{a0[0], a0[1], a1[0], a1[1]}, tr_frag(sm, ta, ta + 512), qa);
    }
    if (G == 0) *(LAS f32x4*)(sm + L_QP + a * 256 + m * 16) = qa;
    lds_signal(sm, L_FLG + 0, lane);
    if (a == 0) {
      const u64 tag = ep + 1ull, tagq = tag << 16;
      lds_wait(sm, L_FLG + 0, 4);
      float qv = 0.f;
#pragma unroll
      for (int i = 0; i < 4; ++i) qv += *(LAS float*)(sm + L_QP + i * 256 + m * 16 + G * 4);
      __hip_atomic_store((GAS u64*)(ws + WS_Q) + (G * 4096 + 512 * g + 16 * r + m), tagq | (u64)(pk2(qv, qv) & 0xffffu), RLX_AGENT);
      STAMP(2);
      {
        const u32x4 e = {SENT, SENT, SENT, SENT};
        __builtin_amdgcn_raw_buffer_store_b128(e, rs, WS_O + slot * 2048 + lane * 16, 0, 16);
        __builtin_amdgcn_raw_buffer_store_b128(e, rs, WS_O + slot * 2048 + 1024 + lane * 16, 0, 16);
        if (lane < 18)
          __builtin_amdgcn_raw_buffer_store_b128(e, rs, lane < 16 ? WS_A + ((b * 8 + g) * 512 + 64 * s) * 4 + lane * 16 : WS_ML + slot * 32 + (lane - 16) * 16, 0, 16);
        if (mlp) {
          __builtin_amdgcn_raw_buffer_store_b128(e, rs, WS_Y + (b * 56 + f) * 2048 + lane * 16, 0, 16);
          __builtin_amdgcn_raw_buffer_store_b128(e, rs, WS_Y + (b * 56 + f) * 2048 + 1024 + lane * 16, 0, 16);
        }
      }
      const GAS u64* qg = (const GAS u64*)(ws + WS_Q) + (b * 4096 + 512 * g + lane);
      u64 qq[8];
      u64 bad = 0;
#pragma unroll
      for (int i = 0; i < 8; ++i) qq[i] = __hip_atomic_load(qg + 64 * i, RLX_AGENT);
#pragma unroll
      for (int i = 0; i < 8; ++i) bad |= qq[i] ^ tagq;
      bool allok = __all((bad >> 16) == 0ull);
      asm volatile("s_waitcnt vmcnt(0)" ::: "memory");
      __hip_atomic_store((GAS u64*)(ws + WS_FLAG) + bid, tag, RLX_AGENT);
      STAMP(3);
#pragma unroll 1
      for (int it = 0; it < SPIN_CAP && !allok; ++it) {
        bad = 0;
#pragma unroll
        for (int i = 0; i < 8; ++i) qq[i] = __hip_atomic_load(qg + 64 * i, RLX_AGENT);
#pragma unroll
        for (int i = 0; i < 8; ++i) bad |= qq[i] ^ tagq;
        allok = __all((bad >> 16) == 0ull);
      }
#pragma unroll
      for (int i = 0; i < 8; ++i) *(LAS u16*)(sm + L_QS + (64 * i + lane) * 2) = (u16)qq[i];
      lds_signal(sm, L_FLG + 4, lane);
      STAMP(4);
      const GAS u64* fg = (const GAS u64*)(ws + WS_FLAG) + lane;
#pragma unroll 1
      for (int it = 0; it < SPIN_CAP; ++it) {
        bool ok = true;
#pragma unroll
        for (int i = 0; i < 4; ++i) ok &= __hip_atomic_load(fg + 64 * i, RLX_AGENT) == tag;
        if (__all(ok)) break;
        __builtin_amdgcn_s_sleep(4);
      }
      if (bid == 0 && lane == 0) __hip_atomic_store((GAS u64*)(ws + WS_EPOCH), tag, RLX_AGENT);
      STAMP(5);
    } else {
      __builtin_amdgcn_s_setprio(0);
      {
        const u16* src = wo + (size_t)(512 * g + (lane >> 3)) * 512 + 64 * s;
        for (int i = a - 1; i < 64; i += 3) {
          const int sg = (G & 1) | ((i & 1) << 1);
          glds(src + (size_t)i * 4096 + 8 * ((lane & 7) ^ (sg << 1)), sm + L_WO + i * 1024);
        }
      }
      if (mlp) {
        const u16* src1 = w1 + (size_t)(lane >> 2) * 1792 + 32 * f;
        for (int i = a - 1; i < 32; i += 3)
          glds(src1 + (size_t)i * 16 * 1792 + 8 * ((lane & 3) ^ (((i >> 3) & 1) << 1)), sm + L_W1 + i * 1024);
        const u16* src2 = w2 + (size_t)(32 * f) * 512;
        for (int i = a - 1; i < 32; i += 3)
          glds(src2 + i * 512 + 8 * (lane ^ (((i & 3) | ((i >> 1) & 4)) << 1)), sm + L_W2 + i * 1024);
      }
      STAMP(2);
      asm volatile("s_waitcnt vmcnt(0)" ::: "memory");
      STAMP(3);
    }
    const u32x2 b2v = *(const u32x2*)(b2 + 16 * (4 * g + (s & 3)) + 4 * (lane & 3));
    WG_BARRIER();
    STAMP(6);
    if (a < 2) {
      f32x4 o = *(LAS f32x4*)(sm + L_MRG + (64 * a + lane) * 16);
#pragma unroll
      for (int w = 1; w < 8; ++w) o += *(LAS f32x4*)(sm + L_MRG + w * 2048 + (64 * a + lane) * 16);
      __builtin_amdgcn_raw_buffer_store_b128(u32x4{clean(o[0]), clean(o[1]), clean(o[2]), clean(o[3])}, rs, WS_O + slot * 2048 + (64 * a + lane) * 16, 0, 16);
    } else if (a == 2 && lane < 4) {
      const f32x4 m0 = *(LAS f32x4*)(sm + L_MX + lane * 32), m1 = *(LAS f32x4*)(sm + L_MX + lane * 32 + 16);
      const f32x4 l0 = *(LAS f32x4*)(sm + L_LX + lane * 32), l1 = *(LAS f32x4*)(sm + L_LX + lane * 32 + 16);
      const float M = fmaxf(fmaxf(fmaxf(m0[0], m0[1]), fmaxf(m0[2], m0[3])), fmaxf(fmaxf(m1[0], m1[1]), fmaxf(m1[2], m1[3])));
      const float L = ((l0[0] + l0[1]) + (l0[2] + l0[3])) + ((l1[0] + l1[1]) + (l1[2] + l1[3]));
      __hip_atomic_store((GAS u64*)(ws + WS_ML + slot * 32 + lane * 8), (u64)clean(M) | ((u64)clean(L) << 32), RLX_AGENT);
    }
    STAMP(7);
    WG_BARRIER();
    STAMP(8);
    if (a == 0) {
      float t = 0.f;
#pragma unroll
      for (int w = 0; w < 8; ++w) t += *(LAS float*)(sm + L_WP + w * 256 + lane * 4);
      __hip_atomic_store((GAS u32*)(ws + WS_A) + ((b * 8 + g) * 512 + 64 * s + lane), clean(t), RLX_AGENT);
    }
    STAMP(9);
    if (mlp) {
      WG_BARRIER();
      WG_BARRIER();
      STAMP(10);
      if (a == 0 && s < 4) {
        const int j = 4 * g + s;
        const u32 yo = WS_Y + ((b * 56 + (lane >> 2)) * 512 + 16 * j + 4 * (lane & 3)) * 4;
        u32x4 y0, y1, y2, y3;
#pragma unroll 1
        for (int it = 0; it < SPIN_CAP; ++it) {
          CFENCE();
          y0 = __builtin_amdgcn_raw_buffer_load_b128(rs, yo, 0, 16);
          y1 = __builtin_amdgcn_raw_buffer_load_b128(rs, yo + 16 * 2048, 0, 16);
          y2 = __builtin_amdgcn_raw_buffer_load_b128(rs, yo + 32 * 2048, 0, 16);
          y3 = u32x4{0u, 0u, 0u, 0u};
          if (lane < 32) y3 = __builtin_amdgcn_raw_buffer_load_b128(rs, yo + 48 * 2048, 0, 16);
          if (__all(max(max(umax4(y0), umax4(y1)), max(umax4(y2), umax4(y3))) != SENT)) break;
        }
        STAMP(11);
        float o[4];
#pragma unroll
        for (int k = 0; k < 4; ++k) {
          float t = (asf(y0[k]) + asf(y1[k])) + (asf(y2[k]) + asf(y3[k]));
          t += dpp<0x124>(t); t += dpp<ROR8>(t);
          o[k] = rows_sum(t);
        }
        const f32x4 hv = *(LAS f32x4*)(sm + L_HB + (16 * j + 4 * (lane & 3)) * 4);
        const float r0 = hv[0] + rbf(rbf(o[0]) + blo(b2v[0])), r1 = hv[1] + rbf(rbf(o[1]) + bhi(b2v[0]));
        const float r2 = hv[2] + rbf(rbf(o[2]) + blo(b2v[1])), r3 = hv[3] + rbf(rbf(o[3]) + bhi(b2v[1]));
        if (lane < 4) *(u32x2*)(out + b * 512 + 16 * j + 4 * lane) = u32x2{pk2(r0, r1), pk2(r2, r3)};
        STAMP(12);
      }
    }
    STAMP_OUT();
    return;
  }

  const int w = wv - 4;
  __builtin_amdgcn_s_setprio(1);
  const size_t kvb = ((size_t)(b * 8 + g) * 4096 + 512 * s + 16 * w) * 128;
  u32x4 kk[4][4], vv[4][4];
  const u32 xh = x[b * 512 + 64 * w + lane];
  const u32 b1v = b1[32 * min(f, 55) + 8 * G + (m & 7)];
  {
    const u16* kp = Kc + kvb + (4 * (m & 3) + (m >> 2)) * 128 + 8 * G;
#pragma unroll
    for (int t = 0; t < 2; ++t)
#pragma unroll
      for (int i = 0; i < 4; ++i) kk[t][i] = __builtin_nontemporal_load((const u32x4*)(kp + t * 16384 + 32 * i));
    __builtin_amdgcn_sched_barrier(0);
    WG_BARRIER();
    __builtin_amdgcn_sched_barrier(0);
#pragma unroll
    for (int t = 2; t < 4; ++t)
#pragma unroll
      for (int i = 0; i < 4; ++i) kk[t][i] = __builtin_nontemporal_load((const u32x4*)(kp + t * 16384 + 32 * i));
    const u16* vp = Vc + kvb + lane * 8;
#pragma unroll
    for (int t = 0; t < 4; ++t) {
      __builtin_amdgcn_sched_barrier(0);
#pragma unroll
      for (int j = 0; j < 4; ++j) vv[t][j] = __builtin_nontemporal_load((const u32x4*)(vp + t * 16384 + 512 * j));
    }
  }
  __builtin_amdgcn_sched_barrier(0);
  STAMP(1);
  lds_wait(sm, L_FLG + 4, 1);
  STAMP(2);
  float sc[4];
  {
    u32x4 qf[4];
#pragma unroll
    for (int i = 0; i < 4; ++i) qf[i] = *(LAS u32x4*)(sm + L_QS + (m >> 2) * 256 + (32 * i + 8 * G) * 2);
#pragma unroll
    for (int t = 0; t < 4; ++t) {
      f32x4 acc = {0.f, 0.f, 0.f, 0.f};
#pragma unroll
      for (int i = 0; i < 4; ++i) acc = mfma(kk[t][i], qf[i], acc);
#pragma unroll
      for (int j = 0; j < 4; ++j) sc[j] = (t == 0 || (m & 3) == t) ? acc[j] : sc[j];
    }
  }
#pragma unroll
  for (int j = 0; j < 4; j += 2) {
    const u32 u = pk2(sc[j], sc[j + 1]);
    const u32 v = pk2(blo(u) * 0.08838834764831845f, bhi(u) * 0.08838834764831845f);
    sc[j] = blo(v); sc[j + 1] = bhi(v);
  }
  float mx = fmaxf(fmaxf(sc[0], sc[1]), fmaxf(sc[2], sc[3]));
  mx = fmaxf(mx, dpp<XOR1>(mx)); mx = fmaxf(mx, dpp<XOR2>(mx)); mx = rows_max(mx);
  if (lane < 16 && (lane & 3) == 0) *(LAS float*)(sm + L_MX + (lane >> 2) * 32 + w * 4) = mx;
  lds_signal(sm, L_FLG + 8, lane);
  STAMP(3);
  lds_wait(sm, L_FLG + 8, 8);
  {
    const f32x4 m0 = *(LAS f32x4*)(sm + L_MX + (m >> 2) * 32), m1 = *(LAS f32x4*)(sm + L_MX + (m >> 2) * 32 + 16);
    mx = fmaxf(fmaxf(fmaxf(m0[0], m0[1]), fmaxf(m0[2], m0[3])), fmaxf(fmaxf(m1[0], m1[1]), fmaxf(m1[2], m1[3])));
  }
  float p[4];
#pragma unroll
  for (int j = 0; j < 4; ++j) p[j] = __builtin_amdgcn_exp2f((sc[j] - mx) * LOG2E);
  {
    float l = (p[0] + p[1]) + (p[2] + p[3]);
    l += dpp<XOR1>(l); l += dpp<XOR2>(l); l = rows_sum(l);
    if (lane < 16 && (lane & 3) == 0) *(LAS float*)(sm + L_LX + (lane >> 2) * 32 + w * 4) = l;
  }
  const u32 p01 = pk2(p[0], p[1]), p23 = pk2(p[2], p[3]);
  STAMP(4);
  {
    f32x4 acc[8];
#pragma unroll
    for (int c = 0; c < 8; ++c) acc[c] = f32x4{0.f, 0.f, 0.f, 0.f};
#pragma unroll
    for (int be = 0; be < 2; ++be) {
      const bool e0 = (m & 3) == 2 * be, e1 = (m & 3) == 2 * be + 1;
      const u32x4 pa = {e0 ? p01 : 0u, e0 ? p23 : 0u, e1 ? p01 : 0u, e1 ? p23 : 0u};
#pragma unroll
      for (int c = 0; c < 8; ++c) {
        const u32 sel = (c & 1) ? 0x07060302u : 0x05040100u;
        const u32x4 vf = {__builtin_amdgcn_perm(vv[2 * be][1][c >> 1], vv[2 * be][0][c >> 1], sel),
                          __builtin_amdgcn_perm(vv[2 * be][3][c >> 1], vv[2 * be][2][c >> 1], sel),
                          __builtin_amdgcn_perm(vv[2 * be + 1][1][c >> 1], vv[2 * be + 1][0][c >> 1], sel),
                          __builtin_amdgcn_perm(vv[2 * be + 1][3][c >> 1], vv[2 * be + 1][2][c >> 1], sel)};
        acc[c] = mfma(pa, vf, acc[c]);
      }
    }
    float o[8];
#pragma unroll
    for (int c = 0; c < 8; ++c) o[c] = (acc[c][0] + acc[c][1]) + (acc[c][2] + acc[c][3]);
    *(LAS f32x4*)(sm + L_MRG + w * 2048 + G * 512 + m * 32) = f32x4{o[0], o[1], o[2], o[3]};
    *(LAS f32x4*)(sm + L_MRG + w * 2048 + G * 512 + m * 32 + 16) = f32x4{o[4], o[5], o[6], o[7]};
  }
  STAMP(5);
  WG_BARRIER();
  STAMP(6);
  u32x4 wof[2][4], w1f[2][2], w2f[4];
  {
    const u32 oo = WS_O + ((b * 8 + g) * 8 + (lane & 7)) * 2048 + (64 * w + 8 * (4 * ((lane >> 3) & 1) + G)) * 4;
    const u32 mo = WS_ML + ((b * 8 + g) * 8 + (lane & 7)) * 32 + (w >> 1) * 8;
    u32x4 o0 = __builtin_amdgcn_raw_buffer_load_b128(rs, oo, 0, 16), o1 = __builtin_amdgcn_raw_buffer_load_b128(rs, oo + 16, 0, 16);
    u32x2 ml = __builtin_amdgcn_raw_buffer_load_b64(rs, mo, 0, 16);
    __builtin_amdgcn_sched_barrier(0);
    {
      const int q = m >> 2, pp = m & 3;
      const int sg = ((q >> 1) & 1) | ((G & 1) << 1);
#pragma unroll
      for (int aa = 0; aa < 2; ++aa)
#pragma unroll
        for (int ct = 0; ct < 4; ++ct) {
          const u32 ta = L_WO + (64 * w + 32 * aa + 8 * G + q) * 128 + ((ct ^ sg) << 5) + 8 * pp;
          wof[aa][ct] = tr_frag(sm, ta, ta + 512);
        }
      if (mlp) {
#pragma unroll
        for (int rr = 0; rr < 2; ++rr)
#pragma unroll
          for (int ct = 0; ct < 2; ++ct) {
            const u32 ta = L_W1 + (128 * G + 8 * (2 * w + rr) + q) * 64 + ((ct ^ (G & 1)) << 5) + 8 * pp;
            w1f[rr][ct] = tr_frag(sm, ta, ta + 256);
          }
#pragma unroll
        for (int ct = 0; ct < 4; ++ct) {
          const u32 ta = L_W2 + (8 * G + q) * 1024 + (((4 * w + ct) ^ (q | ((G & 1) << 2))) << 5) + 8 * pp;
          w2f[ct] = tr_frag(sm, ta, ta + 4096);
        }
      } else {
#pragma unroll
        for (int ct = 0; ct < 4; ++ct) { w2f[ct] = u32x4{0u, 0u, 0u, 0u}; w1f[ct >> 1][ct & 1] = u32x4{0u, 0u, 0u, 0u}; }
      }
    }
    __builtin_amdgcn_sched_barrier(0);
#pragma unroll 1
    for (int it = 0; it < SPIN_CAP; ++it) {
      if (__all(max(max(umax4(o0), umax4(o1)), max(ml[0], ml[1])) != SENT)) break;
      CFENCE();
      o0 = __builtin_amdgcn_raw_buffer_load_b128(rs, oo, 0, 16);
      o1 = __builtin_amdgcn_raw_buffer_load_b128(rs, oo + 16, 0, 16);
      ml = __builtin_amdgcn_raw_buffer_load_b64(rs, mo, 0, 16);
    }
    STAMP(7);
    const float ms = asf(ml[0]);
    float M = fmaxf(ms, dpp<XOR1>(ms)); M = fmaxf(M, dpp<XOR2>(M)); M = fmaxf(M, dpp<XOR7>(M));
    const float e = __builtin_amdgcn_exp2f((ms - M) * LOG2E);
    float L = e * asf(ml[1]);
    L += dpp<XOR1>(L); L += dpp<XOR2>(L); L += dpp<XOR7>(L);
    const float wg = e * __builtin_amdgcn_rcpf(L);
    const u32x4 oa = {pk2(asf(o0[0]) * wg, asf(o0[1]) * wg), pk2(asf(o0[2]) * wg, asf(o0[3]) * wg),
                      pk2(asf(o1[0]) * wg, asf(o1[1]) * wg), pk2(asf(o1[2]) * wg, asf(o1[3]) * wg)};
    const bool hi = (lane & 8) != 0;
    const u32x4 A0 = {hi ? 0u : oa[0], hi ? 0u : oa[1], hi ? 0u : oa[2], hi ? 0u : oa[3]};
    const u32x4 A1 = {hi ? oa[0] : 0u, hi ? oa[1] : 0u, hi ? oa[2] : 0u, hi ? oa[3] : 0u};
    float v[4];
#pragma unroll
    for (int ct = 0; ct < 4; ++ct) {
      f32x4 acc = mfma(A0, wof[0][ct], f32x4{0.f, 0.f, 0.f, 0.f});
      acc = mfma(A1, wof[1][ct], acc);
      v[ct] = (acc[0] + acc[1]) + (acc[2] + acc[3]);
    }
    *(LAS float*)(sm + L_WP + w * 256 + lane * 4) = rows_tr_sum(v[0], v[1], v[2], v[3]);
  }
  STAMP(8);
  WG_BARRIER();
  if (mlp) {
    const u32 ao = WS_A + ((b * 8 + (lane & 7)) * 512 + 64 * w + 8 * (lane >> 3)) * 4;
    u32x4 a0, a1;
#pragma unroll 1
    for (int it = 0; it < SPIN_CAP; ++it) {
      CFENCE();
      a0 = __builtin_amdgcn_raw_buffer_load_b128(rs, ao, 0, 16);
      a1 = __builtin_amdgcn_raw_buffer_load_b128(rs, ao + 16, 0, 16);
      if (__all(max(umax4(a0), umax4(a1)) != SENT)) break;
    }
    STAMP(9);
    {
      const bool c4 = (lane & 4) != 0, c2 = (lane & 2) != 0, c1 = (lane & 1) != 0;
      float t[4];
#pragma unroll
      for (int k = 0; k < 4; ++k) {
        const float keep = c4 ? asf(a1[k]) : asf(a0[k]), send = c4 ? asf(a0[k]) : asf(a1[k]);
        t[k] = keep + dpp<XOR7>(send);
      }
      const float u0 = (c2 ? t[2] : t[0]) + dpp<XOR2>(c2 ? t[0] : t[2]), u1 = (c2 ? t[3] : t[1]) + dpp<XOR2>(c2 ? t[1] : t[3]);
      const float at = (c1 ? u1 : u0) + dpp<XOR1>(c1 ? u0 : u1);
      *(LAS float*)(sm + L_HB + (64 * w + lane) * 4) = rbf(blo(xh) + rbf(at));
    }
    WG_BARRIER();
    STAMP(10);
    float y[8];
    {
      const f32x4 h0 = *(LAS f32x4*)(sm + L_HB + lane * 32), h1 = *(LAS f32x4*)(sm + L_HB + lane * 32 + 16);
      y[0] = h0[0]; y[1] = h0[1]; y[2] = h0[2]; y[3] = h0[3]; y[4] = h1[0]; y[5] = h1[1]; y[6] = h1[2]; y[7] = h1[3];
    }
    ln8(y);
    ln8(y);
    const u32x4 ya = {pk2(y[0], y[1]), pk2(y[2], y[3]), pk2(y[4], y[5]), pk2(y[6], y[7])};
    const bool r0 = m == 2 * w, r1 = m == 2 * w + 1;
    const u32x4 Y0 = {r0 ? ya[0] : 0u, r0 ? ya[1] : 0u, r0 ? ya[2] : 0u, r0 ? ya[3] : 0u};
    const u32x4 Y1 = {r1 ? ya[0] : 0u, r1 ? ya[1] : 0u, r1 ? ya[2] : 0u, r1 ? ya[3] : 0u};
#pragma unroll
    for (int ct = 0; ct < 2; ++ct) {
      f32x4 acc = mfma(Y0, w1f[0][ct], f32x4{0.f, 0.f, 0.f, 0.f});
      acc = mfma(Y1, w1f[1][ct], acc);
      if (G == (w >> 1)) *(LAS float*)(sm + L_HP + w * 128 + (16 * ct + m) * 4) = (acc[0] + acc[1]) + (acc[2] + acc[3]);
    }
    STAMP(11);
    WG_BARRIER();
    float hs = 0.f;
#pragma unroll
    for (int k = 0; k < 8; ++k) hs += *(LAS float*)(sm + L_HP + k * 128 + (8 * G + (m & 7)) * 4);
    const float u = rbf(rbf(hs) + blo(b1v));
    const float z2 = u * (1.5957691216057308f + 0.07135481282600173f * u * u);
    const float ge = u * __builtin_amdgcn_rcpf(1.f + __builtin_amdgcn_exp2f(-z2 * LOG2E));
    const u32 hb = (m & 1) ? (pk2(ge, ge) & 0xffff0000u) : (pk2(ge, ge) & 0xffffu);
    const u32x4 HA = {(m >> 1) == 0 ? hb : 0u, (m >> 1) == 1 ? hb : 0u, (m >> 1) == 2 ? hb : 0u, (m >> 1) == 3 ? hb : 0u};
    float v[4];
#pragma unroll
    for (int ct = 0; ct < 4; ++ct) {
      const f32x4 acc = mfma(HA, w2f[ct], f32x4{0.f, 0.f, 0.f, 0.f});
      v[ct] = (acc[0] + acc[1]) + (acc[2] + acc[3]);
    }
    __hip_atomic_store((GAS u32*)(ws + WS_Y) + ((b * 56 + f) * 512 + 64 * w + lane), clean(rows_tr_sum(v[0], v[1], v[2], v[3])), RLX_AGENT);
    STAMP(12);
  }
  STAMP_OUT();
}

extern "C" void kernel_launch(void* const* d_in, const int* in_sizes, int n_in, void* d_out, int out_size, void* d_ws,
                              size_t ws_size, hipStream_t stream) {
  (void)in_sizes; (void)n_in; (void)out_size; (void)ws_size;
  (void)hipFuncSetAttribute((const void*)fused_decode, hipFuncAttributeMaxDynamicSharedMemorySize, L_END);
  fused_decode<<<256, 768, L_END, stream>>>((const u16*)d_in[0], (const u16*)d_in[1], (const u16*)d_in[2], (const u16*)d_in[3],
                                            (const u16*)d_in[4], (const u16*)d_in[6], (const u16*)d_in[7], (const u16*)d_in[8],
                                            (const u16*)d_in[9], (u16*)d_out, (unsigned char*)d_ws);
}
```

```cpp
#include <hip/hip_runtime.h>
#include <stdint.h>

typedef unsigned u32;
typedef unsigned long long u64;
typedef unsigned short u16;
typedef u32 u32x4 __attribute__((ext_vector_type(4)));
typedef u32 u32x2 __attribute__((ext_vector_type(2)));
typedef float f32x4 __attribute__((ext_vector_type(4)));
typedef __bf16 bf16x8 __attribute__((ext_vector_type(8)));
typedef __bf16 bf16x2 __attribute__((ext_vector_type(2)));
typedef float f32x2 __attribute__((ext_vector_type(2)));
typedef short s16x4 __attribute__((ext_vector_type(4)));
#define LAS __attribute__((address_space(3)))
#define GAS __attribute__((address_space(1)))
#define RLX_AGENT __ATOMIC_RELAXED, __HIP_MEMORY_SCOPE_AGENT

constexpr int AREP = 4;
constexpr u32 WS_EPOCH = 0, WS_FLAG = 4096, WS_TK = 8192, WS_Q = 16384, WS_O = WS_Q + 524288, WS_ML = WS_O + 262144,
              WS_A = WS_ML + 32768, WS_Y = WS_A + AREP * 131072, WS_END = WS_Y + 112 * 2048;
constexpr u32 L_W1 = 0, L_XN = L_W1, L_WO = 8192, L_WP = 40960, L_W2 = 65536, L_WQ = 114688, L_MRG = L_WQ, L_HP = 131072, L_CSP = L_HP + 2048, L_ST = L_CSP + 2048, L_HB = L_HP + 8192,
              L_GB = L_HB + 2048, L_QS = L_GB + 1024, L_MX = L_QS + 1024, L_LX = L_MX + 128, L_FLG = L_LX + 128, L_END = L_FLG + 64;
constexpr u32 F_XN = L_FLG, F_MID = L_FLG + 4, F_QRDY = L_FLG + 8, F_MX = L_FLG + 12, F_MRD = L_FLG + 16, F_VISS = L_FLG + 20, F_WO = L_FLG + 24,
              F_W1 = L_FLG + 28, F_W2 = L_FLG + 48, F_CHK = L_FLG + 52, F_CS = L_FLG + 56, F_CLR = L_FLG + 60, F_T1 = L_FLG + 32, F_T2 = L_FLG + 36, F_TASK = L_FLG + 40, L_TASK = L_FLG + 44;
constexpr u32 SENT = 0xFFFFFFFFu;
constexpr int SPIN_CAP = 1 << 16;
constexpr float LOG2E = 1.4426950408889634f;

__device__ __forceinline__ float asf(u32 u) { return __builtin_bit_cast(float, u); }
__device__ __forceinline__ u32 asu(float f) { return __builtin_bit_cast(u32, f); }
__device__ __forceinline__ u32 pk2(float lo, float hi) {
  f32x2 v = {lo, hi};
  return __builtin_bit_cast(u32, __builtin_convertvector(v, bf16x2));
}
__device__ __forceinline__ float rbf(float f) { return asf(pk2(f, f) << 16); }
__device__ __forceinline__ float blo(u32 u) { return asf(u << 16); }
__device__ __forceinline__ float bhi(u32 u) { return asf(u & 0xffff0000u); }
template <int CTRL> __device__ __forceinline__ float dpp(float x) {
  return __builtin_bit_cast(float, __builtin_amdgcn_mov_dpp(__builtin_bit_cast(int, x), CTRL, 0xf, 0xf, true));
}
constexpr int XOR1 = 0xB1, XOR2 = 0x4E, XOR7 = 0x141, ROR8 = 0x128;
__device__ __forceinline__ float rows_sum(float x) {
  auto s = __builtin_amdgcn_permlane16_swap(asu(x), asu(x), false, false);
  x = asf(s[0]) + asf(s[1]);
  auto t = __builtin_amdgcn_permlane32_swap(asu(x), asu(x), false, false);
  return asf(t[0]) + asf(t[1]);
}
__device__ __forceinline__ float rows_max(float x) {
  auto s = __builtin_amdgcn_permlane16_swap(asu(x), asu(x), false, false);
  x = fmaxf(asf(s[0]), asf(s[1]));
  auto t = __builtin_amdgcn_permlane32_swap(asu(x), asu(x), false, false);
  return fmaxf(asf(t[0]), asf(t[1]));
}
__device__ __forceinline__ float wave_sum(float x) {
  x += dpp<XOR1>(x); x += dpp<XOR2>(x); x += dpp<XOR7>(x); x += dpp<ROR8>(x);
  return rows_sum(x);
}
__device__ __forceinline__ float rows_tr_sum(float v0, float v1, float v2, float v3) {
  auto a = __builtin_amdgcn_permlane32_swap(asu(v0), asu(v2), false, false);
  auto b = __builtin_amdgcn_permlane32_swap(asu(v1), asu(v3), false, false);
  const float d = asf(a[0]) + asf(a[1]), s = asf(b[0]) + asf(b[1]);
  auto c = __builtin_amdgcn_permlane16_swap(asu(d), asu(s), false, false);
  return asf(c[0]) + asf(c[1]);
}
__device__ __forceinline__ f32x4 mfma(u32x4 a, u32x4 b, f32x4 c) {
  return __builtin_amdgcn_mfma_f32_16x16x32_bf16(__builtin_bit_cast(bf16x8, a), __builtin_bit_cast(bf16x8, b), c, 0, 0, 0);
}
__device__ __forceinline__ u32x4 tr_frag(LAS unsigned char* sm, u32 a0, u32 a1) {
  const s16x4 lo = __builtin_amdgcn_ds_read_tr16_b64_v4i16((LAS s16x4*)(sm + a0));
  const s16x4 hi = __builtin_amdgcn_ds_read_tr16_b64_v4i16((LAS s16x4*)(sm + a1));
  const u32x2 l = __builtin_bit_cast(u32x2, lo), h = __builtin_bit_cast(u32x2, hi);
  return u32x4{l[0], l[1], h[0], h[1]};
}
#define WG_BARRIER() asm volatile("s_waitcnt lgkmcnt(0)\n\ts_barrier" ::: "memory")
#define CFENCE() asm volatile("" ::: "memory")
__device__ __forceinline__ void lds_wait(LAS unsigned char* sm, u32 off, u32 target) {
#pragma unroll 1
  for (int it = 0; it < (SPIN_CAP << 4); ++it) {
    if (*(volatile LAS u32*)(sm + off) >= target) break;
    __builtin_amdgcn_s_sleep(1);
  }
  CFENCE();
}
__device__ __forceinline__ void lds_spin(LAS unsigned char* sm, u32 off, u32 target) {
#pragma unroll 1
  for (int it = 0; it < (SPIN_CAP << 6); ++it)
    if (*(volatile LAS u32*)(sm + off) >= target) break;
  CFENCE();
}
__device__ __forceinline__ u32 lds_arrive(LAS unsigned char* sm, u32 off, int lane) {
  u32 v; u64 keep;
  asm volatile("s_waitcnt lgkmcnt(0)\n\ts_mov_b64 %1, exec\n\ts_mov_b64 exec, 1\n\tds_add_rtn_u32 %0, %2, %3\n\ts_mov_b64 exec, %1\n\ts_waitcnt lgkmcnt(0)"
               : "=&v"(v), "=&s"(keep) : "v"((u32)(size_t)sm + off), "v"(1u) : "memory");
  return __builtin_amdgcn_readfirstlane(v);
}
__device__ __forceinline__ void lds_signal(LAS unsigned char* sm, u32 off, int lane) {
  u64 keep;
  asm volatile("s_waitcnt lgkmcnt(0)\n\ts_mov_b64 %0, exec\n\ts_mov_b64 exec, 1\n\tds_add_u32 %1, %2\n\ts_mov_b64 exec, %0"
               : "=&s"(keep) : "v"((u32)(size_t)sm + off), "v"(1u) : "memory");
}

constexpr int NT = 2, SC1 = 16;
constexpr u32 CODE_BYTES = 15872;
#define VMWAIT(n) asm volatile("s_waitcnt vmcnt(%0)" :: "n"(n) : "memory")
typedef __amdgpu_buffer_rsrc_t rsrc_t;
__device__ __forceinline__ rsrc_t mkrs(const void* p, u32 bytes) { return __builtin_amdgcn_make_buffer_rsrc((void*)p, 0, bytes, 0x00020000); }
template <int AUX> __device__ __forceinline__ void dma(rsrc_t r, LAS unsigned char* l, u32 voff, u32 soff) {
  __builtin_amdgcn_raw_ptr_buffer_load_lds(r, (LAS void*)l, 16, voff, soff, 0, AUX);
}
__device__ __forceinline__ float max8(f32x4 a, f32x4 b) {
  return fmaxf(fmaxf(fmaxf(a[0], a[1]), fmaxf(a[2], a[3])), fmaxf(fmaxf(b[0], b[1]), fmaxf(b[2], b[3])));
}
__device__ __forceinline__ float sum4(f32x4 a) { const f32x2 t = f32x2{a[0], a[1]} + f32x2{a[2], a[3]}; return t[0] + t[1]; }
__device__ __forceinline__ u32 qbc(u32 x, int t) {
  return t == 0 ? __builtin_amdgcn_mov_dpp(x, 0x00, 0xf, 0xf, true) : t == 1 ? __builtin_amdgcn_mov_dpp(x, 0x55, 0xf, 0xf, true)
       : t == 2 ? __builtin_amdgcn_mov_dpp(x, 0xAA, 0xf, 0xf, true) : __builtin_amdgcn_mov_dpp(x, 0xFF, 0xf, 0xf, true);
}
#define PIN(x) asm volatile("" : "+v"(x))
constexpr u32 OOB = 0x7ffffff0u;
__device__ __forceinline__ u32 umax4(u32x4 v) { return max(max(v[0], v[1]), max(v[2], v[3])); }
__device__ __forceinline__ u32 clean(float f) { return min(asu(f), SENT - 1u); }
template <bool RELEASE> __device__ __forceinline__ void pace(LAS unsigned char* sm, u32 tstart, u32 n) {
#pragma unroll 1
  while ((u32)__builtin_amdgcn_s_memrealtime() - tstart < 85u + n * 57u) {
    if (RELEASE) {
      const u64 t = *(volatile LAS u64*)(sm + F_TASK);
      if (__builtin_amdgcn_readfirstlane((u32)t != 0u && (u32)(t >> 32) < 4u)) break;
    }
    __builtin_amdgcn_s_sleep(1);
  }
}
__device__ __forceinline__ void ln8(float (&v)[8]) {
  float s = ((v[0] + v[1]) + (v[2] + v[3])) + ((v[4] + v[5]) + (v[6] + v[7]));
  const float mean = wave_sum(s) * (1.f / 512.f);
  float q = 0.f;
#pragma unroll
  for (int j = 0; j < 8; ++j) { const float d = v[j] - mean; q = fmaf(d, d, q); }
  const float var = wave_sum(q) * (1.f / 512.f);
  const float mb = rbf(mean);
  const float sd = rbf(__builtin_amdgcn_sqrtf(rbf(rbf(var) + 1e-5f)));
  const float inv = __builtin_amdgcn_rcpf(sd);
#pragma unroll
  for (int j = 0; j < 8; j += 2) {
    const u32 d = pk2(v[j] - mb, v[j + 1] - mb);
    const u32 y = pk2(blo(d) * inv, bhi(d) * inv);
    v[j] = blo(y); v[j + 1] = bhi(y);
  }
}

__global__ void __launch_bounds__(768, 3)
fused_decode(const u16* __restrict__ x, const u16* __restrict__ Kc, const u16* __restrict__ Vc, const u16* __restrict__ wq,
             const u16* __restrict__ wo, const u16* __restrict__ w1, const u16* __restrict__ b1, const u16* __restrict__ w2,
             const u16* __restrict__ b2, u16* __restrict__ out, unsigned char* ws) {
  extern __shared__ __attribute__((aligned(16))) unsigned char smem_[];
  LAS unsigned char* sm = (LAS unsigned char*)smem_;
  const int bid = blockIdx.x, g = bid & 7, r = bid >> 3, b = r >> 3, s = r & 7;
  const int slot = (b * 8 + g) * 8 + s;
  const int lane = threadIdx.x & 63, wv = __builtin_amdgcn_readfirstlane(threadIdx.x >> 6);
  const int m = lane & 15, G = lane >> 4;
  const bool aux = wv < 4;
  const int w = wv - (aux ? 0 : 4), a = wv;
  const rsrc_t rs = mkrs(ws, WS_END);
  const f32x4 zero4 = {0.f, 0.f, 0.f, 0.f};
  const u32 aclr = WS_A + (lane >> 5) * 131072 + ((((lane >> 3) & 3) * 32 + 4 * g + b) * 512 + 64 * s + 8 * (lane & 7)) * 2;
  const u32 kvb = ((b * 8 + g) * 4096 + 512 * s + 16 * w) * 256;
  const u32 ko = kvb + (4 * (lane & 3) + ((lane >> 2) & 1)) * 256 + 64 * ((lane >> 3) & 1) + 16 * G, vo = kvb + lane * 16;
  const u32 tstart = (u32)__builtin_amdgcn_s_memrealtime();
  u32x4 xr = {0u, 0u, 0u, 0u};
  u32 cpf = 0;
  u32x2 epl = {0u, 0u};
  if (aux) {
    __builtin_amdgcn_s_setprio(3);
    if (lane < 16) *(LAS u32*)(sm + L_FLG + 4 * lane) = 0u;
    xr = *(const u32x4*)(x + a * 512 + lane * 8);
    const rsrc_t rq = mkrs(wq, 512 * 4096 * 2);
    const u32 vq = (128 * b + 32 * a + (lane >> 3)) * 8192 + (512 * g + 64 * s) * 2 + 16 * ((lane & 7) ^ (((lane >> 4) & 3) << 1));
#pragma unroll
    for (int i = 0; i < 4; ++i) dma<0>(rq, sm + L_WQ + (32 * a + 8 * i) * 128, vq, i * 8 * 8192);
    epl = __builtin_amdgcn_raw_buffer_load_b64(rs, WS_EPOCH, 0, SC1);
    __builtin_amdgcn_sched_barrier(0);
    {
      u64 pc;
      asm volatile("s_getpc_b64 %0" : "=s"(pc));
      pc &= ~255ull;
      cpf = *(const GAS u32*)(pc + min((u32)(((r & 7) * 16 + a * 4 + (lane & 3)) * 128), CODE_BYTES - 4u));
    }
  }
  __builtin_amdgcn_sched_barrier(0);
  WG_BARRIER();
  __builtin_amdgcn_sched_barrier(0);

  if (__builtin_expect(!aux, 1)) {
    __builtin_amdgcn_s_setprio(1);
    const rsrc_t rk = mkrs(Kc, 4 * 8 * 4096 * 256), rv = mkrs(Vc, 4 * 8 * 4096 * 256);
    u32x4 kk[4][4], vv[4][4];
    f32x4 ew;
    const int hcol = 64 * w + 32 * (G & 1) + 16 * (G >> 1) + m;
    if (__builtin_expect(w < 4, 1)) {
#pragma unroll
      for (int t = 0; t < 4; ++t) {
#pragma unroll
        for (int i = 0; i < 4; ++i) kk[t][i] = __builtin_amdgcn_raw_buffer_load_b128(rk, ko + (i >> 1) * 512 + (i & 1) * 128, t * 32768, NT);
        __builtin_amdgcn_sched_barrier(0);
      }
    } else {
      typedef const unsigned char* cp;
      cp p0 = (cp)Vc + kvb, p1 = p0, p2 = p0, p3 = p0;
      if (w == 5) { p0 = (cp)wo + (4 * g + b) * 131072; p1 = p0 + 65536; p2 = (cp)b2; p3 = p2; }
      if (w == 6) { p0 = (cp)w1 + (r & 7) * 65536; p1 = p0 + 8 * 65536; p2 = p0 + 16 * 65536; p3 = (cp)w1 + min(24 + (r & 7), 27) * 65536; }
      if (w == 7) { p0 = (cp)w2 + min(g + 8 * (r & 3), 27) * 65536; p1 = (cp)b1; p2 = p0; p3 = p0; }
      u32 t0, t1, t2, t3;
      asm volatile("s_load_dword %0, %4, 0x0\n\ts_load_dword %1, %5, 0x0\n\ts_load_dword %2, %6, 0x0\n\ts_load_dword %3, %7, 0x0\n\ts_waitcnt lgkmcnt(0)"
                   : "=&s"(t0), "=&s"(t1), "=&s"(t2), "=&s"(t3) : "s"(p0), "s"(p1), "s"(p2), "s"(p3) : "memory");
#pragma unroll
      for (int t = 0; t < 4; ++t) {
        pace<false>(sm, tstart, 8 + t);
        __builtin_amdgcn_sched_barrier(0);
#pragma unroll
        for (int i = 0; i < 4; ++i) kk[t][i] = __builtin_amdgcn_raw_buffer_load_b128(rk, ko + (i >> 1) * 512 + (i & 1) * 128, t * 32768, NT);
        __builtin_amdgcn_sched_barrier(0);
      }
    }
#pragma unroll
    for (int t = 0; t < 4; ++t) {
      pace<true>(sm, tstart, (w >= 4 ? 12 : 4) + t);
      __builtin_amdgcn_sched_barrier(0);
#pragma unroll
      for (int j = 0; j < 4; ++j) vv[t][j] = __builtin_amdgcn_raw_buffer_load_b128(rv, vo + 1024 * j, t * 32768, NT);
      if (t == 0) lds_signal(sm, F_MID, lane);
      __builtin_amdgcn_sched_barrier(0);
    }
    lds_signal(sm, F_VISS, lane);
    lds_wait(sm, F_QRDY, 2);
    float sc[4];
    {
      u32x4 qf[2];
#pragma unroll
      for (int i = 0; i < 2; ++i) qf[i] = *(LAS u32x4*)(sm + L_QS + (m >> 2) * 256 + (64 * i + 32 * (m & 1) + 8 * G) * 2);
#pragma unroll
      for (int t = 0; t < 4; ++t) {
        const f32x4 c0 = mfma(kk[t][1], qf[1], mfma(kk[t][0], qf[0], zero4));
        const f32x4 c1 = mfma(kk[t][3], qf[1], mfma(kk[t][2], qf[0], zero4));
#pragma unroll
        for (int j = 0; j < 4; ++j) {
          auto sw = __builtin_amdgcn_permlane32_swap(asu(c0[j]), asu(c1[j]), false, false);
          const float v = (t & 1) ? asf(sw[1]) + dpp<XOR1>(asf(sw[0])) : asf(sw[0]) + dpp<XOR1>(asf(sw[1]));
          sc[j] = (t == 0 || (m & 3) == t) ? v : sc[j];
        }
      }
    }
#pragma unroll
    for (int j = 0; j < 4; j += 2) {
      const u32 u = pk2(sc[j], sc[j + 1]);
      const u32 v = pk2(blo(u) * 0.08838834764831845f, bhi(u) * 0.08838834764831845f);
      sc[j] = blo(v); sc[j + 1] = bhi(v);
    }
    float mx = fmaxf(fmaxf(sc[0], sc[1]), fmaxf(sc[2], sc[3]));
    mx = fmaxf(mx, dpp<XOR1>(mx)); mx = fmaxf(mx, dpp<XOR2>(mx)); mx = rows_max(mx);
    float p[4];
#pragma unroll
    for (int j = 0; j < 4; ++j) p[j] = __builtin_amdgcn_exp2f((sc[j] - mx) * LOG2E);
    {
      float l = (p[0] + p[1]) + (p[2] + p[3]);
      l += dpp<XOR1>(l); l += dpp<XOR2>(l); l = rows_sum(l);
      if (lane < 16 && (lane & 3) == 0) {
        *(LAS float*)(sm + L_MX + (lane >> 2) * 32 + w * 4) = mx;
        *(LAS float*)(sm + L_LX + (lane >> 2) * 32 + w * 4) = l;
      }
      lds_signal(sm, F_MX, lane);
    }
    const u32 p01 = pk2(p[0], p[1]), p23 = pk2(p[2], p[3]);
    const int q4 = m >> 2, pp = m & 3, ks = w & 3;
    const int sl = ((2 * (w >> 2) + (m >> 3)) * 8 + g) * 8 + (m & 7);
    u32 a_mw = L_MRG + w * 2048 + G * 512 + m * 32, a_mr = L_MRG + (lane >> 5) * 8192 + (64 * w + 4 * m) * 4;
    u32 a_op = (lane & 0x31) == 0 ? WS_O + slot * 1024 + (64 * w + 4 * m) * 2 : OOB;
    u32 oo = WS_O + sl * 1024 + (128 * b + 32 * ks + 8 * G) * 2, mo = WS_ML + sl * 128 + b * 8;
    u32 a_wp = L_WP + ks * 2048 + (2 * (w >> 2) + (G >> 1)) * 512 + (16 * (G & 1) + m) * 4, a_wpr = L_WP + lane * 32;
    u32 a_wo = L_WO + (32 * ks + 8 * G + q4) * 256 + ((q4 | ((G & 1) << 2)) << 5) + 8 * pp;
    f32x4 acc[8];
#pragma unroll
    for (int be = 0; be < 2; ++be) {
      const u32x4 pa = {qbc(p01, 2 * be), qbc(p23, 2 * be), qbc(p01, 2 * be + 1), qbc(p23, 2 * be + 1)};
      u32x4 vf[8];
#pragma unroll
      for (int k = 0; k < 4; ++k) {
#pragma unroll
        for (int c = 0; c < 8; ++c)
          vf[c][k] = __builtin_amdgcn_perm(vv[2 * be + (k >> 1)][2 * (k & 1) + 1][c >> 1], vv[2 * be + (k >> 1)][2 * (k & 1)][c >> 1], (c & 1) ? 0x07060302u : 0x05040100u);
        if (k < 3) __builtin_amdgcn_sched_barrier(0);
      }
#pragma unroll
      for (int c = 0; c < 8; ++c) acc[c] = mfma(pa, vf[c], be == 0 ? zero4 : acc[c]);
      __builtin_amdgcn_sched_barrier(0);
      if (be == 0) {
        lds_wait(sm, F_MX, 8);
        lds_wait(sm, F_CLR, 4);
        const int h = w >> 1;
        const f32x4 m0 = *(LAS f32x4*)(sm + L_MX + h * 32), m1 = *(LAS f32x4*)(sm + L_MX + h * 32 + 16);
        const float M = max8(m0, m1);
#pragma unroll
        for (int k = 0; k < 4; ++k) ew[k] = __builtin_amdgcn_exp2f(((lane < 32 ? m0[k] : m1[k]) - M) * LOG2E);
        u32x2 mlv = {0u, 0u};
        if (w == 0) {
          const int hh = lane & 3;
          const f32x4 n0 = *(LAS f32x4*)(sm + L_MX + hh * 32), n1 = *(LAS f32x4*)(sm + L_MX + hh * 32 + 16);
          const f32x4 l0 = *(LAS f32x4*)(sm + L_LX + hh * 32), l1 = *(LAS f32x4*)(sm + L_LX + hh * 32 + 16);
          const float N = max8(n0, n1);
          float L = 0.f;
#pragma unroll
          for (int k = 0; k < 4; ++k) L = fmaf(__builtin_amdgcn_exp2f((n0[k] - N) * LOG2E), l0[k], fmaf(__builtin_amdgcn_exp2f((n1[k] - N) * LOG2E), l1[k], L));
          mlv = u32x2{clean(N), clean(L)};
        }
        __builtin_amdgcn_raw_buffer_store_b64(mlv, rs, (w == 0 && lane < 4) ? WS_ML + slot * 128 + lane * 8 : OOB, 0, SC1);
        PIN(a_mw); PIN(a_mr); PIN(a_op); PIN(oo); PIN(mo); PIN(a_wp); PIN(a_wpr); PIN(a_wo);
        __builtin_amdgcn_sched_barrier(0);
      }
    }
    *(LAS f32x4*)(sm + a_mw) = f32x4{acc[0][0], acc[1][0], acc[2][0], acc[3][0]};
    *(LAS f32x4*)(sm + a_mw + 16) = f32x4{acc[4][0], acc[5][0], acc[6][0], acc[7][0]};
    lds_signal(sm, F_T1, lane);
    lds_spin(sm, F_T1, 8);
    {
      const f32x4 o = (ew[0] * *(LAS f32x4*)(sm + a_mr) + ew[1] * *(LAS f32x4*)(sm + a_mr + 2048)) +
                      (ew[2] * *(LAS f32x4*)(sm + a_mr + 4096) + ew[3] * *(LAS f32x4*)(sm + a_mr + 6144));
      float z[4];
#pragma unroll
      for (int k = 0; k < 4; ++k) {
        auto y = __builtin_amdgcn_permlane32_swap(asu(o[k]), asu(o[k]), false, false);
        z[k] = asf(y[0]) + asf(y[1]);
      }
      const u32 u0 = min(pk2(z[0], z[1]), SENT - 1u), u1 = min(pk2(z[2], z[3]), SENT - 1u);
      __builtin_amdgcn_raw_buffer_store_b128(u32x4{u0, u1, (u32)__builtin_amdgcn_mov_dpp(u0, XOR1, 0xf, 0xf, true), (u32)__builtin_amdgcn_mov_dpp(u1, XOR1, 0xf, 0xf, true)},
                                             rs, a_op, 0, SC1);
      lds_signal(sm, F_MRD, lane);
    }
    lds_spin(sm, F_TASK, 1);
    const u32 rank = __builtin_amdgcn_readfirstlane(*(LAS u32*)(sm + L_TASK));
    if (rank >= 4u) {
      u32 apub = WS_A + ((G * 32 + 4 * g + b) * 512 + 128 * (rank - 4) + 8 * m) * 2, bpa = 16 * G;
      PIN(apub); PIN(bpa);
      lds_spin(sm, F_CHK, 1);
      lds_spin(sm, F_MRD, 8);
      u32x2 ml = __builtin_amdgcn_raw_buffer_load_b64(rs, mo, 0, SC1);
      u32x4 oa = __builtin_amdgcn_raw_buffer_load_b128(rs, oo, 0, SC1);
      __builtin_amdgcn_sched_barrier(0);
      u32x4 wof[8];
      lds_spin(sm, F_WO, 4);
#pragma unroll
      for (int ct = 0; ct < 8; ++ct) wof[ct] = tr_frag(sm, a_wo ^ (ct << 5), (a_wo ^ (ct << 5)) + 1024);
      __builtin_amdgcn_sched_barrier(0);
      float wr[4] = {0.f, 0.f, 0.f, 0.f};
      bool have = false;
#pragma unroll 1
      for (int it = 0; it < SPIN_CAP; ++it) {
        const bool n0 = max(ml[0], ml[1]) == SENT, n1 = umax4(oa) == SENT;
        const bool a0 = __any(n0), a1 = __any(n1);
        float ms = asf(ml[0]), ls = asf(ml[1]);
        PIN(ms); PIN(ls);
        CFENCE();
        if (n1) oa = __builtin_amdgcn_raw_buffer_load_b128(rs, oo, 0, SC1);
        if (n0) ml = __builtin_amdgcn_raw_buffer_load_b64(rs, mo, 0, SC1);
        if (!have && !a0) {
          float M = fmaxf(ms, dpp<XOR1>(ms)); M = fmaxf(M, dpp<XOR2>(M)); M = fmaxf(M, dpp<XOR7>(M));
          const float e = __builtin_amdgcn_exp2f((ms - M) * LOG2E);
          float L = e * ls;
          L += dpp<XOR1>(L); L += dpp<XOR2>(L); L += dpp<XOR7>(L);
          const float wg = e * __builtin_amdgcn_rcpf(L);
#pragma unroll
          for (int k = 0; k < 4; ++k) wr[k] = asf(__builtin_amdgcn_ds_bpermute(bpa + 4 * k, asu(wg)));
          have = true;
        }
        if (!a0 && !a1) break;
      }
      f32x4 pd[8];
#pragma unroll
      for (int ct = 0; ct < 8; ++ct) pd[ct] = mfma(oa, wof[ct], zero4);
#pragma unroll
      for (int j = 0; j < 4; ++j) {
        const float v0 = fmaf(pd[2 * j][0], wr[0], pd[2 * j][1] * wr[1]) + fmaf(pd[2 * j][2], wr[2], pd[2 * j][3] * wr[3]);
        const float v1 = fmaf(pd[2 * j + 1][0], wr[0], pd[2 * j + 1][1] * wr[1]) + fmaf(pd[2 * j + 1][2], wr[2], pd[2 * j + 1][3] * wr[3]);
        auto sp = __builtin_amdgcn_permlane16_swap(asu(v0), asu(v1), false, false);
        *(LAS float*)(sm + a_wp + 128 * j) = asf(sp[0]) + asf(sp[1]);
      }
      if (lds_arrive(sm, F_T2, lane) == 7) {
        const f32x4 t0 = (*(LAS f32x4*)(sm + a_wpr) + *(LAS f32x4*)(sm + a_wpr + 2048)) + (*(LAS f32x4*)(sm + a_wpr + 4096) + *(LAS f32x4*)(sm + a_wpr + 6144));
        const f32x4 t1 = (*(LAS f32x4*)(sm + a_wpr + 16) + *(LAS f32x4*)(sm + a_wpr + 2064)) + (*(LAS f32x4*)(sm + a_wpr + 4112) + *(LAS f32x4*)(sm + a_wpr + 6160));
        const u32x4 av = {min(pk2(t0[0], t0[1]), SENT - 1u), min(pk2(t0[2], t0[3]), SENT - 1u), min(pk2(t1[0], t1[1]), SENT - 1u), min(pk2(t1[2], t1[3]), SENT - 1u)};
#pragma unroll
        for (int i = 0; i < AREP; ++i) __builtin_amdgcn_raw_buffer_store_b128(av, rs, apub, i * 131072, SC1);
      }
    } else if (8 * rank + g < 28) {
      const u32 task = 4 * (8 * rank + g) + b;
      const int bt = b, f = task >> 2;
      const u32 xh = x[bt * 512 + hcol];
      const u32 b1v = b1[64 * f + lane];
      const u32 ao = WS_A + (g & (AREP - 1)) * 131072 + ((bt * 32 + (lane >> 3)) * 512 + 64 * w + 8 * (lane & 7)) * 2;
      u32x4 w1f[2][4], w2f[2][4];
      lds_spin(sm, F_CHK, 1);
      u32x4 ag[4];
#pragma unroll
      for (int i = 0; i < 4; ++i) ag[i] = u32x4{SENT, 0u, 0u, 0u};
      const u32 a_w2 = L_W2 + (8 * G + q4) * 1024 + (((4 * w) ^ (q4 | ((G & 1) << 2))) << 5) + 8 * pp;
      const int k2 = m >> 1;
      const u32 half = (m & 1) ? 0xffff0000u : 0xffffu;
      u32x4 sel, hm0, hm1;
#pragma unroll
      for (int k = 0; k < 4; ++k) {
        sel[k] = (k2 & 3) == k ? (half & 0x3f803f80u) : 0u; hm0[k] = k2 == k ? half : 0u; hm1[k] = k2 == k + 4 ? half : 0u;
      }
      PIN(sel); PIN(hm0); PIN(hm1);
      u32 a_hp = L_HP + w * 256 + lane * 4, a_hb = L_HB + hcol * 4, a_hr = L_HP + lane * 4, a_gw = L_GB + w * 128 + lane * 2, a_gr = L_GB + w * 128 + G * 16;
      u32 a_yp = m < 4 ? WS_Y + task * 2048 + (64 * w + 16 * m + 4 * G) * 4 : OOB, a_st = L_ST + w * 8 + 4 * G, a_sr = L_ST;
      PIN(a_hp); PIN(a_hb); PIN(a_hr); PIN(a_gw); PIN(a_gr); PIN(a_yp); PIN(a_st); PIN(a_sr);
      lds_spin(sm, F_W1, 4);
      {
        const u32 ta = L_W1 + (64 * w + 32 * (G & 1) + 16 * (G >> 1) + q4) * 128 + ((((q4 >> 1) & 1) | ((G & 1) << 1)) << 5) + 8 * pp;
#pragma unroll
        for (int aa = 0; aa < 2; ++aa)
#pragma unroll
          for (int ct = 0; ct < 4; ++ct) w1f[aa][ct] = tr_frag(sm, (ta ^ (ct << 5)) + aa * 1024, (ta ^ (ct << 5)) + aa * 1024 + 512);
      }
      const u32x4 ones = {0x3f803f80u, 0x3f803f80u, 0x3f803f80u, 0x3f803f80u};
      float cs[4];
#pragma unroll
      for (int ct = 0; ct < 4; ++ct) cs[ct] = mfma(ones, w1f[1][ct], mfma(ones, w1f[0][ct], zero4))[0];
      *(LAS float*)(sm + a_hp + 2048) = G < 2 ? (G == 0 ? cs[0] : cs[1]) : (G == 2 ? cs[2] : cs[3]);
      lds_signal(sm, F_CS, lane);
      float xf = blo(xh), b1f = blo(b1v);
      PIN(xf); PIN(b1f);
      lds_spin(sm, F_W2, 4);
#pragma unroll
      for (int aa = 0; aa < 2; ++aa)
#pragma unroll
        for (int ct = 0; ct < 4; ++ct) w2f[aa][ct] = tr_frag(sm, (a_w2 ^ (ct << 5)) + aa * 32768, (a_w2 ^ (ct << 5)) + aa * 32768 + 4096);
      lds_spin(sm, F_CS, 8);
      float csum = 0.f;
#pragma unroll
      for (int k = 0; k < 8; ++k) csum += *(LAS float*)(sm + a_hr + 2048 + k * 256);
      PIN(csum);
      __builtin_amdgcn_sched_barrier(0);
#pragma unroll 1
      for (int it = 0; it < SPIN_CAP; ++it) {
        const bool n0 = umax4(ag[0]) == SENT, n1 = umax4(ag[1]) == SENT, n2 = umax4(ag[2]) == SENT, n3 = umax4(ag[3]) == SENT;
        if (!__any(n0 | n1 | n2 | n3)) break;
        CFENCE();
        if (n0) ag[0] = __builtin_amdgcn_raw_buffer_load_b128(rs, ao, 0, SC1);
        if (n1) ag[1] = __builtin_amdgcn_raw_buffer_load_b128(rs, ao, 8192, SC1);
        if (n2) ag[2] = __builtin_amdgcn_raw_buffer_load_b128(rs, ao, 16384, SC1);
        if (n3) ag[3] = __builtin_amdgcn_raw_buffer_load_b128(rs, ao, 24576, SC1);
      }
      {
        const f32x4 hs = mfma(ag[3], sel, mfma(ag[2], sel, mfma(ag[1], sel, mfma(ag[0], sel, zero4))));
        auto x0 = __builtin_amdgcn_permlane32_swap(asu(hs[0]), asu(hs[2]), false, false);
        auto x1 = __builtin_amdgcn_permlane32_swap(asu(hs[1]), asu(hs[3]), false, false);
        const float at = (m & 8) ? asf(x1[0]) + asf(x1[1]) : asf(x0[0]) + asf(x0[1]);
        const float hx = xf + rbf(at);
        const u32 hd = pk2(hx, hx);
        const float hv = blo(hd);
        const u32x4 H0 = {hd & hm0[0], hd & hm0[1], hd & hm0[2], hd & hm0[3]}, H1 = {hd & hm1[0], hd & hm1[1], hd & hm1[2], hd & hm1[3]};
        __builtin_amdgcn_sched_barrier(0);
        f32x4 d[4];
        float s1 = hv, s2 = hv * hv;
#define GAP() __builtin_amdgcn_sched_barrier(0)
        d[0] = mfma(H0, w1f[0][0], zero4); s1 += dpp<XOR1>(s1); s2 += dpp<XOR1>(s2); GAP();
        d[1] = mfma(H0, w1f[0][1], zero4); s1 += dpp<XOR2>(s1); s2 += dpp<XOR2>(s2); GAP();
        d[2] = mfma(H0, w1f[0][2], zero4); s1 += dpp<XOR7>(s1); s2 += dpp<XOR7>(s2); GAP();
        d[3] = mfma(H0, w1f[0][3], zero4); s1 += dpp<ROR8>(s1); s2 += dpp<ROR8>(s2); GAP();
        d[0] = mfma(H1, w1f[1][0], d[0]);
        auto t16 = __builtin_amdgcn_permlane16_swap(asu(s1), asu(s2), false, false);
        float ss = asf(t16[0]) + asf(t16[1]); GAP();
        d[1] = mfma(H1, w1f[1][1], d[1]);
        auto t32 = __builtin_amdgcn_permlane32_swap(asu(ss), asu(ss), false, false);
        ss = asf(t32[0]) + asf(t32[1]); GAP();
        d[2] = mfma(H1, w1f[1][2], d[2]);
        if ((lane & 47) == 0) *(LAS float*)(sm + a_st) = ss;
        GAP();
        d[3] = mfma(H1, w1f[1][3], d[3]);
        *(LAS float*)(sm + a_hb) = hv;
        GAP();
        *(LAS float*)(sm + a_hp) = rows_tr_sum(sum4(d[0]), sum4(d[1]), sum4(d[2]), sum4(d[3]));
      }
      WG_BARRIER();
      const f32x4 sa = *(LAS f32x4*)(sm + a_sr), sb = *(LAS f32x4*)(sm + a_sr + 16), sc4 = *(LAS f32x4*)(sm + a_sr + 32), sd = *(LAS f32x4*)(sm + a_sr + 48);
      float hp[8];
#pragma unroll
      for (int k = 0; k < 8; ++k) hp[k] = *(LAS float*)(sm + a_hr + k * 256);
      __builtin_amdgcn_sched_barrier(0);
      const f32x4 st4 = (sa + sb) + (sc4 + sd);
      const float mu = (st4[0] + st4[2]) * (1.f / 512.f);
      const float var = fmaxf((st4[1] + st4[3]) * (1.f / 512.f) - mu * mu, 0.f);
      const float rr = __builtin_amdgcn_rsqf(fmaf(var, 1.00001f, 1e-10f));
      const f32x2 h2 = (f32x2{hp[0], hp[1]} + f32x2{hp[2], hp[3]}) + (f32x2{hp[4], hp[5]} + f32x2{hp[6], hp[7]});
      const float hsum = h2[0] + h2[1];
      const float u = rbf(rbf((hsum - mu * csum) * rr) + b1f);
      const float z2 = u * (1.5957691216057308f + 0.07135481282600173f * u * u);
      const float ge = u * __builtin_amdgcn_rcpf(1.f + __builtin_amdgcn_exp2f(-z2 * LOG2E));
      *(LAS u16*)(sm + a_gw) = (u16)pk2(ge, ge);
      const u32x4 g0 = *(LAS u32x4*)(sm + a_gr), g1 = *(LAS u32x4*)(sm + a_gr + 64);
      f32x4 yd[4];
#pragma unroll
      for (int ct = 0; ct < 4; ++ct) yd[ct] = mfma(w2f[0][ct], g0, zero4);
#pragma unroll
      for (int ct = 0; ct < 4; ++ct) yd[ct] = mfma(w2f[1][ct], g1, yd[ct]);
      u32x4 yv;
#pragma unroll
      for (int k = 0; k < 4; ++k) yv[k] = clean((m & 2) ? ((m & 1) ? yd[3][k] : yd[2][k]) : ((m & 1) ? yd[1][k] : yd[0][k]));
      __builtin_amdgcn_raw_buffer_store_b128(yv, rs, a_yp, 0, SC1);
    }
  } else {
    {
      float v[8];
#pragma unroll
      for (int j = 0; j < 4; ++j) { v[2 * j] = blo(xr[j]); v[2 * j + 1] = bhi(xr[j]); }
      ln8(v);
      *(LAS u32x4*)(sm + L_XN + a * 1024 + lane * 16) = u32x4{pk2(v[0], v[1]), pk2(v[2], v[3]), pk2(v[4], v[5]), pk2(v[6], v[7])};
    }
    asm volatile("s_waitcnt vmcnt(1)" : "+v"(epl) :: "memory");
    lds_signal(sm, F_XN, lane);
    const u64 tag = (((u64)epl[1] << 32) | epl[0]) + 1ull;
    lds_spin(sm, F_XN, 4);
    f32x4 qa = zero4;
#pragma unroll
    for (int ks = 0; ks < 4; ++ks) {
      const int k0 = 32 * ks + 4 * G;
      const u32 xa = L_XN + (m >> 2) * 1024 + (128 * b + k0) * 2;
      const u32x2 a0 = *(LAS u32x2*)(sm + xa), a1 = *(LAS u32x2*)(sm + xa + 32);
      const u32 ta = L_WQ + (k0 + (m >> 2)) * 128 + ((a ^ (((G & 1) << 1) | (m >> 3))) << 5) + 8 * (m & 3);
      qa = mfma(u32x4{a0[0], a0[1], a1[0], a1[1]}, tr_frag(sm, ta, ta + 2048), qa);
    }
    __hip_atomic_store((GAS u64*)(ws + WS_Q) + (((G * 8 + g) * 4 + b) * 512 + 64 * s + 16 * a + m), (tag << 32) | (u64)asu(qa[0]), RLX_AGENT);
    {
      const u32x4 e = {SENT, SENT, SENT, SENT};
      const u32 o0 = a == 3 ? WS_O + slot * 1024 + lane * 16 : (lane < 56 ? WS_Y + bid * 896 + lane * 16 : lane < 58 ? WS_ML + slot * 128 + (lane - 56) * 16 : OOB);
      if (a >= 2) __builtin_amdgcn_raw_buffer_store_b128(e, rs, o0, 0, SC1);
      else __builtin_amdgcn_raw_buffer_store_b128(e, rs, aclr + a * 262144, 0, SC1);
    }
    if (a == 0 && s == 0 && lane == 0) __hip_atomic_store((GAS u64*)(ws + WS_TK + (b * 8 + g) * 256), tag << 24, RLX_AGENT);
    VMWAIT(0);
    asm volatile("" :: "v"(cpf));
    if (lds_arrive(sm, F_CLR, lane) == 3 && lane == 0) __hip_atomic_store((GAS u64*)(ws + WS_FLAG) + bid, tag, RLX_AGENT);
    if (a < 2) {
      lds_wait(sm, F_VISS, 3);
      const u32 tl = (u32)tag;
      const u32 qo = WS_Q + ((b * 8 + g) * 2048 + 256 * a + 2 * lane) * 8;
      u32x4 qg[8];
#pragma unroll
      for (int i = 0; i < 8; ++i) qg[i] = __builtin_amdgcn_raw_buffer_load_b128(rs, qo + (i & 1) * 1024, (i >> 1) * 4096, SC1);
#pragma unroll 1
      for (int it = 0; it < SPIN_CAP; ++it) {
        u32 bad = 0u;
#pragma unroll
        for (int i = 0; i < 8; ++i) bad |= (((qg[i][1] ^ tl) | (qg[i][3] ^ tl)) != 0u) << i;
        if (!__any(bad != 0u)) break;
        __builtin_amdgcn_s_sleep(8);
        CFENCE();
#pragma unroll
        for (int i = 0; i < 8; ++i)
          if (bad >> i & 1) qg[i] = __builtin_amdgcn_raw_buffer_load_b128(rs, qo + (i & 1) * 1024, (i >> 1) * 4096, SC1);
      }
#pragma unroll
      for (int i = 0; i < 2; ++i) {
        const float lo = (asf(qg[i][0]) + asf(qg[i + 2][0])) + (asf(qg[i + 4][0]) + asf(qg[i + 6][0]));
        const float hi = (asf(qg[i][2]) + asf(qg[i + 2][2])) + (asf(qg[i + 4][2]) + asf(qg[i + 6][2]));
        *(LAS u32*)(sm + L_QS + (256 * a + 128 * i + 2 * lane) * 2) = pk2(lo, hi);
      }
      lds_signal(sm, F_QRDY, lane);
    }
    if (a == 3) {
      lds_wait(sm, F_QRDY, 2);
      const u32 fl = (u32)tag, fh = (u32)(tag >> 32);
#pragma unroll 1
      for (int it = 0; it < SPIN_CAP; ++it) {
        CFENCE();
        const u32x4 f0 = __builtin_amdgcn_raw_buffer_load_b128(rs, WS_FLAG + lane * 16, 0, SC1), f1 = __builtin_amdgcn_raw_buffer_load_b128(rs, WS_FLAG + lane * 16, 1024, SC1);
        if (__all(((f0[0] ^ fl) | (f0[1] ^ fh) | (f0[2] ^ fl) | (f0[3] ^ fh) | (f1[0] ^ fl) | (f1[1] ^ fh) | (f1[2] ^ fl) | (f1[3] ^ fh)) == 0u)) break;
        __builtin_amdgcn_s_sleep(16);
      }
      lds_signal(sm, F_CHK, lane);
    } else if (a == 2) {
      lds_wait(sm, F_MID, 5);
      u64 tk = 0;
#pragma unroll 1
      for (int it = 0; it < SPIN_CAP; ++it) {
        if (lane == 0) tk = __hip_atomic_fetch_add((GAS u64*)(ws + WS_TK + (b * 8 + g) * 256), 1ull, RLX_AGENT);
        if (__builtin_amdgcn_readfirstlane(((tk ^ (tag << 24)) >> 24) == 0ull)) break;
        __builtin_amdgcn_s_sleep(16);
      }
      const u32 n = (u32)tk & 0xffffffu;
      if (lane == 0) *(LAS u32*)(sm + L_TASK) = n;
      lds_signal(sm, F_TASK, lane);
    }
    VMWAIT(0);
    __builtin_amdgcn_s_setprio(2);
    lds_wait(sm, F_CLR, 4);
    lds_wait(sm, F_VISS, 8);
    lds_wait(sm, F_TASK, 1);
    const u32 rank = __builtin_amdgcn_readfirstlane(*(LAS u32*)(sm + L_TASK));
    if (rank >= 4u) {
      const rsrc_t ro = mkrs(wo, 4096 * 512 * 2);
      const u32 vo = (128 * (4 * g + b) + 4 * a + G) * 1024 + 256 * (rank - 4) + 16 * (m ^ ((G | ((2 * a) & 4)) << 1));
#pragma unroll 1
      for (int k = 0; k < 8; ++k) dma<NT>(ro, sm + L_WO + (4 * k + a) * 1024, vo, k * 16384);
      VMWAIT(0);
      lds_signal(sm, F_WO, lane);
    } else if (8 * rank + g < 28) {
      const u32 task = 4 * (8 * rank + g) + b;
      const int bt = b, f = task >> 2;
      const rsrc_t r1 = mkrs(w1, 512 * 1792 * 2), r2 = mkrs(w2, 512 * 1792 * 2);
      const u32 v1 = (8 * a + (lane >> 3)) * 3584 + 128 * f + 16 * ((lane & 7) ^ ((G & 1) << 1));
#pragma unroll 1
      for (int k = 0; k < 16; ++k) {
        VMWAIT(5);
        dma<0>(r1, sm + L_W1 + (4 * k + a) * 1024, v1 ^ ((k & 1) << 6), k * 32 * 3584);
      }
#pragma unroll 1
      for (int k = 0; k < 16; ++k) {
        if (k == 6) { VMWAIT(6); lds_signal(sm, F_W1, lane); }
        if (k == 12) lds_wait(sm, F_MRD, 8);
        VMWAIT(15);
        dma<0>(r2, sm + L_W2 + (4 * k + a) * 1024, 16 * (lane ^ ((a | ((k & 2) << 1)) << 1)), (64 * f + 4 * k + a) * 1024);
      }
      VMWAIT(0);
      lds_signal(sm, F_W2, lane);
      if (a == 3 && f < 16) {
        __builtin_amdgcn_s_setprio(3);
        const int c0 = 32 * f + 4 * (lane & 7);
        const int cf = c0 + 2 * ((lane >> 3) & 1) + (G & 1);
        const u32 b2v = b2[cf];
        const u32 yo = WS_Y + (4 * (lane >> 3) + bt) * 2048 + c0 * 4;
        WG_BARRIER();
        float hv = *(LAS float*)(sm + L_HB + cf * 4), b2f = blo(b2v);
        u16* op = out + bt * 512 + cf;
        PIN(hv); PIN(b2f); PIN(op);
        if (task == 0 && lane == 0) __hip_atomic_store((GAS u64*)(ws + WS_EPOCH), tag, RLX_AGENT);
        __builtin_amdgcn_s_sleep(20);
        u32x4 y0 = {SENT, 0u, 0u, 0u}, y1 = y0, y2 = y0, y3 = {lane < 32 ? SENT : 0u, 0u, 0u, 0u};
#pragma unroll 1
        for (int it = 0; it < SPIN_CAP; ++it) {
          const bool n0 = umax4(y0) == SENT, n1 = umax4(y1) == SENT, n2 = umax4(y2) == SENT, n3 = umax4(y3) == SENT;
          if (!__any(n0 | n1 | n2 | n3)) break;
          CFENCE();
          if (n0) y0 = __builtin_amdgcn_raw_buffer_load_b128(rs, yo, 0, SC1);
          if (n1) y1 = __builtin_amdgcn_raw_buffer_load_b128(rs, yo, 32 * 2048, SC1);
          if (n2) y2 = __builtin_amdgcn_raw_buffer_load_b128(rs, yo, 64 * 2048, SC1);
          if (n3) y3 = __builtin_amdgcn_raw_buffer_load_b128(rs, yo, 96 * 2048, SC1);
        }
        const f32x4 t = (__builtin_bit_cast(f32x4, y0) + __builtin_bit_cast(f32x4, y1)) + (__builtin_bit_cast(f32x4, y2) + __builtin_bit_cast(f32x4, y3));
        const bool c8 = (lane & 8) != 0;
        const float t0 = (c8 ? t[2] : t[0]) + dpp<ROR8>(c8 ? t[0] : t[2]), t1 = (c8 ? t[3] : t[1]) + dpp<ROR8>(c8 ? t[1] : t[3]);
        auto z16 = __builtin_amdgcn_permlane16_swap(asu(t0), asu(t1), false, false);
        const float z = asf(z16[0]) + asf(z16[1]);
        auto z32 = __builtin_amdgcn_permlane32_swap(asu(z), asu(z), false, false);
        const float res = hv + rbf(rbf(asf(z32[0]) + asf(z32[1])) + b2f);
        if (lane < 32) __hip_atomic_store((GAS u16*)op, (u16)pk2(res, res), RLX_AGENT);
      }
    }
  }
}

extern "C" void kernel_launch(void* const* d_in, const int* in_sizes, int n_in, void* d_out, int out_size, void* d_ws,
                              size_t ws_size, hipStream_t stream) {
  (void)in_sizes; (void)n_in; (void)out_size; (void)ws_size;
  (void)hipFuncSetAttribute((const void*)fused_decode, hipFuncAttributeMaxDynamicSharedMemorySize, L_END);
  fused_decode<<<256, 768, L_END, stream>>>((const u16*)d_in[0], (const u16*)d_in[1], (const u16*)d_in[2], (const u16*)d_in[3],
                                            (const u16*)d_in[4], (const u16*)d_in[6], (const u16*)d_in[7], (const u16*)d_in[8],
                                            (const u16*)d_in[9], (u16*)d_out, (unsigned char*)d_ws);
}
```
